```python
import torch

def _softmax(x, axis=-1):
    return torch.softmax(x, dim=axis)
def _gelu(x):
    return torch.nn.functional.gelu(x, approximate="tanh")
def _silu(x):
    return torch.nn.functional.silu(x)
def _sigmoid(x):
    return torch.sigmoid(x)
def _layernorm(x, axis=-1, eps=1e-5):
    m = x.mean(dim=axis, keepdim=True)
    v = x.var(dim=axis, keepdim=True, unbiased=False)
    return (x - m) / torch.sqrt(v + eps)
def _scatter_add(tgt, idx, src, axis=0):
    return tgt.clone().index_add_(axis, idx, src)
def _segment_sum(src, seg, n):
    out = torch.zeros((n,) + src.shape[1:], dtype=src.dtype)
    return out.index_add_(0, seg, src)
def _pad(x, pw):
    flat = tuple(v for pair in reversed(pw) for v in pair)
    return torch.nn.functional.pad(x, flat)

kernel_name = 'sequential_preLN__gqa_block_cached__swiglu_ffn__decode'

def setup_inputs():

    _w = (lambda a: (lambda t: t.to(torch.bfloat16) if t.is_floating_point() else t)(torch.as_tensor(a)))
    def _A_setup():
        B, Sq, D = 4, 1, 640
        Hq, Hkv, Sk, Dh = 32, 32, 4096, 192
        assert Hq % Hkv == 0, (Hq, Hkv)
        return {
            "x": _w((lambda *s: torch.randn(*s, dtype=torch.bfloat16) * 0.05)(B, Sq, D)),
            "k_cache": _w((lambda *s: torch.randn(*s, dtype=torch.bfloat16) * 3.2)(B, Hkv, Sk, Dh)),
            "v_cache": _w((lambda *s: torch.randn(*s, dtype=torch.bfloat16) * 0.4)(B, Hkv, Sk, Dh)),
            "wq": _w((lambda *s: torch.randn(*s, dtype=torch.bfloat16) * 3.2)(D, Hq * Dh) * (D ** -0.5)),
            "wo": _w((lambda *s: torch.randn(*s, dtype=torch.bfloat16) * 0.4)(Hq * Dh, D) * ((Hq * Dh) ** -0.5)),
        }
    _A_in = _A_setup()

    def _B_setup():
        B, S, D, F = 4, 1, 640, 1536
        return {
            "x": _w((lambda *s: torch.randn(*s, dtype=torch.bfloat16) * 0.05)(B, S, D)),
            "w_gate": _w((lambda *s: torch.randn(*s, dtype=torch.bfloat16) * 0.05)(D, F)),
            "w_up": _w((lambda *s: torch.randn(*s, dtype=torch.bfloat16) * 0.05)(D, F)),
            "w_down": _w((lambda *s: torch.randn(*s, dtype=torch.bfloat16) * 0.05)(F, D)),
        }
    _B_in = _B_setup()

    return {
        "A_x": _A_in["x"],
        "A_k_cache": _A_in["k_cache"],
        "A_v_cache": _A_in["v_cache"],
        "A_wq": _A_in["wq"],
        "A_wo": _A_in["wo"],
        "B_x": _B_in["x"],
        "B_w_gate": _B_in["w_gate"],
        "B_w_up": _B_in["w_up"],
        "B_w_down": _B_in["w_down"],
    }

def reference(A_x, A_k_cache, A_v_cache, A_wq, A_wo, B_x, B_w_gate, B_w_up, B_w_down):
    def _A_ref(x, k_cache, v_cache, wq, wo):
        B, Sq, D = x.shape
        Hq, Dh = 32, 192
        Hkv = k_cache.shape[1]
        rep = Hq // Hkv
        q = (lambda x, *d: x.permute(*d))((x @ wq).reshape(B, Sq, Hq, Dh), 0, 2, 1, 3)
        k = (lambda x, n, axis: torch.repeat_interleave(x, n, dim=axis))(k_cache, rep, axis=1)
        v = (lambda x, n, axis: torch.repeat_interleave(x, n, dim=axis))(v_cache, rep, axis=1)
        scores = torch.einsum("bhqd,bhkd->bhqk", q, k) * (Dh ** -0.5)
        p = _softmax(scores, axis=-1)
        o = torch.einsum("bhqk,bhkd->bhqd", p, v)
        return (lambda x, *d: x.permute(*d))(o, 0, 2, 1, 3).reshape(B, Sq, Hq * Dh) @ wo

    def _B_ref(x, w_gate, w_up, w_down):
        g = _silu(x @ w_gate)
        u = x @ w_up
        return (g * u) @ w_down

    x = A_x
    h = x + _A_ref(_layernorm(x, axis=-1), A_k_cache, A_v_cache, A_wq, A_wo)
    h2 = h + _B_ref(_layernorm(h, axis=-1), B_w_gate, B_w_up, B_w_down)
    return h2
```

```cpp
#include <hip/hip_runtime.h>
#include <stdint.h>

#define KV_AUX 2
#define W_AUX 2
#define NT 8
#define NPRE 2
#define PSHIFT 1
#define PRO_BAR 0
#define OUT_SC 0
#define PSLEEP 1
#define STAMPS 0

typedef unsigned v4u __attribute__((ext_vector_type(4)));
typedef unsigned v2u __attribute__((ext_vector_type(2)));
typedef float f32x4 __attribute__((ext_vector_type(4)));
typedef float f32x2 __attribute__((ext_vector_type(2)));
typedef __bf16 bf16x2 __attribute__((ext_vector_type(2)));
typedef unsigned short u16;
typedef short s16x4 __attribute__((ext_vector_type(4)));
typedef __bf16 bf16x8 __attribute__((ext_vector_type(8)));
#define GAS __attribute__((address_space(1)))
#define LAS __attribute__((address_space(3)))
typedef __amdgpu_buffer_rsrc_t rsrc_t;

constexpr int D = 640, H = 32, SK = 4096, DH = 192, F = 1536;
constexpr int NWG = 256, NTHR = 512;
constexpr int ROWB = DH * 2  , TILEB = 8 * ROWB  , CHB = 8 * TILEB  ;
constexpr int AUX_SC1 = 16, RSRC_W3 = 0x00020000, OOB = 0x7ffffff0;
constexpr unsigned SPIN_CAP = 20000u;
constexpr float LOG2E = 1.4426950408889634f;
constexpr int WS_E = 0;
constexpr int WS_QG = 4096;
constexpr int WS_OG = WS_QG + 32 * 4 * 8 * 128;
constexpr int WS_P2 = WS_OG + 32 * 8 * 104 * 16;
constexpr int WS_HG = WS_P2 + 4 * 40 * 32 * 128;
constexpr int WS_AG = WS_HG + 4 * 320 * 8;
constexpr int WS_END = WS_AG + 2 * 96 * 256;
constexpr int WS_STAMP = 4 << 20;
constexpr int OS_STR = 400, X2_STR = 1296, AS_STR = 3088;
constexpr int L_XS = 0;
constexpr int L_PART = L_XS + 640 * 16;
constexpr int L_QS = L_PART + 192 * 8 * 4;
constexpr int L_P = L_QS + 192 * 2 + 16;
constexpr int L_ML = L_P + 8 * 320 * 4;
constexpr int L_OS = L_ML + 64;
constexpr int L_RED = L_OS + 4 * OS_STR;
constexpr int L_ACT = L_RED + 8 * 64 * 4;
constexpr int L_WR = L_ACT + 4 * AS_STR;
constexpr int L_END = L_WR + 2 * 640 * 64;
static_assert(L_END <= 160 * 1024, "LDS");

__device__ __forceinline__ float dot2(unsigned a, unsigned b, float c) {
  return __builtin_amdgcn_fdot2_f32_bf16(__builtin_bit_cast(bf16x2, a), __builtin_bit_cast(bf16x2, b), c, false);
}
__device__ __forceinline__ float bflo(unsigned u) { return __builtin_bit_cast(float, u << 16); }
__device__ __forceinline__ float bfhi(unsigned u) { return __builtin_bit_cast(float, u & 0xffff0000u); }
__device__ __forceinline__ unsigned bf16bits(float f) { return (unsigned)__builtin_bit_cast(u16, (__bf16)f); }
__device__ __forceinline__ float rbf(float f) { return (float)(__bf16)f; }
__device__ __forceinline__ unsigned packbf(float lo, float hi) {
  bf16x2 v = {(__bf16)lo, (__bf16)hi}; return __builtin_bit_cast(unsigned, v);
}
template <int CTRL> __device__ __forceinline__ float dpp(float x) {
  return __builtin_bit_cast(float, __builtin_amdgcn_mov_dpp(__builtin_bit_cast(int, x), CTRL, 0xf, 0xf, true));
}
template <int CTRL> __device__ __forceinline__ unsigned dppu(unsigned x) { return (unsigned)__builtin_amdgcn_mov_dpp((int)x, CTRL, 0xf, 0xf, true); }
constexpr int XOR1 = 0xB1, XOR2 = 0x4E, XOR7 = 0x141, XOR8 = 0x128;
__device__ __forceinline__ float red32(float a, float b) {
  auto r = __builtin_amdgcn_permlane32_swap(__float_as_uint(a), __float_as_uint(b), false, false);
  return __uint_as_float(r[0]) + __uint_as_float(r[1]);
}
__device__ __forceinline__ float red16(float a, float b) {
  auto r = __builtin_amdgcn_permlane16_swap(__float_as_uint(a), __float_as_uint(b), false, false);
  return __uint_as_float(r[0]) + __uint_as_float(r[1]);
}
template <int CTRL> __device__ __forceinline__ float tred(float lo, float hi, bool sel) {
  const float keep = sel ? hi : lo, send = sel ? lo : hi; return keep + dpp<CTRL>(send);
}
__device__ __forceinline__ float wave_sum(float x) {
  x += dpp<XOR1>(x); x += dpp<XOR2>(x); x += dpp<XOR7>(x); x += dpp<XOR8>(x);
  x = red16(x, x); x = red32(x, x); return x;
}
__device__ __forceinline__ float wave_max(float x) {
  x = fmaxf(x, dpp<XOR1>(x)); x = fmaxf(x, dpp<XOR2>(x)); x = fmaxf(x, dpp<XOR7>(x)); x = fmaxf(x, dpp<XOR8>(x));
  { auto r = __builtin_amdgcn_permlane16_swap(__float_as_uint(x), __float_as_uint(x), false, false); x = fmaxf(__uint_as_float(r[0]), __uint_as_float(r[1])); }
  { auto r = __builtin_amdgcn_permlane32_swap(__float_as_uint(x), __float_as_uint(x), false, false); x = fmaxf(__uint_as_float(r[0]), __uint_as_float(r[1])); }
  return x;
}
__device__ __forceinline__ rsrc_t mk(const void* p, unsigned bytes) { return __builtin_amdgcn_make_buffer_rsrc((void*)p, 0, (int)bytes, RSRC_W3); }
template <int AUX> __device__ __forceinline__ v4u ld128(rsrc_t r, int voff, int soff) { return __builtin_amdgcn_raw_buffer_load_b128(r, voff, soff, AUX); }
__device__ __forceinline__ f32x4 mfma(v4u a, v4u b, f32x4 c) {
  return __builtin_amdgcn_mfma_f32_16x16x32_bf16(__builtin_bit_cast(bf16x8, a), __builtin_bit_cast(bf16x8, b), c, 0, 0, 0);
}
__device__ __forceinline__ v4u bfrag(const LAS unsigned char* p, int stride) {
  const v2u lo = __builtin_bit_cast(v2u, __builtin_amdgcn_ds_read_tr16_b64_v4i16((LAS s16x4*)p));
  const v2u hi = __builtin_bit_cast(v2u, __builtin_amdgcn_ds_read_tr16_b64_v4i16((LAS s16x4*)(p + 4 * stride)));
  return v4u{lo[0], lo[1], hi[0], hi[1]};
}
__device__ __forceinline__ float spread4(f32x4 c) {
  const auto a = __builtin_amdgcn_permlane16_swap(__float_as_uint(c[0]), __float_as_uint(c[1]), false, false);
  const auto d = __builtin_amdgcn_permlane16_swap(__float_as_uint(c[2]), __float_as_uint(c[3]), false, false);
  const auto r = __builtin_amdgcn_permlane32_swap(a[0], d[0], false, false);
  return __uint_as_float(r[0]);
}
#define SB() __builtin_amdgcn_sched_barrier(0)
#define LDS_FENCE() asm volatile("s_waitcnt lgkmcnt(0)" ::: "memory")

__device__ __forceinline__ void fma_tile(f32x2 (&acc)[12], const v4u (&t)[3], float p) {
  const f32x2 pp = {p, p};
#pragma unroll
  for (int j = 0; j < 3; ++j)
#pragma unroll
    for (int k = 0; k < 4; ++k) { const f32x2 v = {bflo(t[j][k]), bfhi(t[j][k])}; acc[j * 4 + k] = __builtin_elementwise_fma(pp, v, acc[j * 4 + k]); }
  asm volatile("" : "+v"(acc[0]), "+v"(acc[1]), "+v"(acc[2]), "+v"(acc[3]), "+v"(acc[4]), "+v"(acc[5]), "+v"(acc[6]), "+v"(acc[7]), "+v"(acc[8]), "+v"(acc[9]), "+v"(acc[10]), "+v"(acc[11]));
}
template <int N> __device__ __forceinline__ void ln_stats(const float (&v)[N], const bool (&valid)[N], float& mb, float& sb, float& rs) {
  const float x0 = __builtin_bit_cast(float, __builtin_amdgcn_readfirstlane(__builtin_bit_cast(int, v[0])));
  float s1 = 0.f, s2 = 0.f;
#pragma unroll
  for (int i = 0; i < N; ++i) { const float d = valid[i] ? v[i] - x0 : 0.f; s1 += d; s2 = fmaf(d, d, s2); }
  const float m1 = wave_sum(s1) * (1.0f / D), m2 = wave_sum(s2) * (1.0f / D);
  mb = rbf(x0 + m1);
  sb = rbf(__builtin_amdgcn_sqrtf(rbf(rbf(fmaxf(fmaf(-m1, m1, m2), 0.f)) + 1e-5f)));
  rs = __builtin_amdgcn_rcpf(sb);
}
__device__ __forceinline__ float fdiv(float a, float b, float rb) { const float q = a * rb; return fmaf(fmaf(-q, b, a), rb, q); }
#define STAMP(i) do {} while (0)
#define STAMP_FLUSH() do {} while (0)

template <int N> __device__ __forceinline__ void gather(rsrc_t rs, const int (&off)[N], bool act, unsigned epoch, v4u (&o)[N]) {
  for (unsigned spins = 0;; ++spins) {
    asm volatile("" ::: "memory");
    bool ok = true;
#pragma unroll
    for (int k = 0; k < N; ++k) o[k] = ld128<AUX_SC1>(rs, off[k], 0);
#pragma unroll
    for (int k = 0; k < N; ++k) ok = ok & (o[k][1] == epoch) & (o[k][3] == epoch);
    if (__all(ok | !act) || spins > SPIN_CAP) break;
    if (PSLEEP) __builtin_amdgcn_s_sleep(PSLEEP);
  }
  asm volatile("" ::: "memory");
}

__global__ void __launch_bounds__(NTHR, 2)
block_kernel(const u16* __restrict__ x, const u16* __restrict__ kc, const u16* __restrict__ vc, const u16* __restrict__ wq,
             const u16* __restrict__ wo, const u16* __restrict__ wg, const u16* __restrict__ wu, const u16* __restrict__ wd,
             u16* __restrict__ out, unsigned char* __restrict__ ws) {
  __shared__ __attribute__((aligned(16))) unsigned char smem[L_END];
  LAS unsigned char* L = (LAS unsigned char*)smem;
  const int bid = blockIdx.x, tid = threadIdx.x, lane = tid & 63, w = __builtin_amdgcn_readfirstlane(tid >> 6);
  const rsrc_t rws = mk(ws, WS_END);
  STAMP(0);
  const int ri = bid >> 3, xl = bid & 7, sp = xl & 1;
  const int unit = (((sp ? xl + 5 : xl) & 7) >> 1) * 32 + ri, h = unit >> 2, b = unit & 3;
  const int j = 2 * b + sp, js = 4 * sp + b;
  const int g = lane >> 3, sub = lane & 7;
  const bool isW = b < 2 + (1 - sp), isR = b < 2 && ri < 20;
  const int ws5 = sp ? 3 + b : b;
  const int rk_ = sp ? (ri >> 2) * 2 + b - 2 : 10 + (ri >> 2);
  const bool isF2 = !isW && rk_ < 10, isF1 = !isW && rk_ >= 10 && rk_ < 16;
  const int f0 = 64 * ((xl >> 1) * 6 + rk_ - 10), c0 = 16 * ((xl >> 1) * 10 + rk_);
  const size_t kvoff = (size_t)(b * H + h) * SK * DH;
  const rsrc_t rk = mk(kc + kvoff, SK * ROWB), rv = mk(vc + kvoff, SK * ROWB);
  const int loff = g * ROWB + sub * 16, cw = (sp ? 63 - w : w) * CHB, cstep = sp ? -8 * CHB : 8 * CHB;
  v4u R[NT][3];
#define LDK0(t) _Pragma("unroll") for (int jj = 0; jj < 3; ++jj) R[t][jj] = ld128<KV_AUX>(rk, loff + jj * 128, cw + (t) * TILEB)

  unsigned epoch;
  {
    const unsigned e0 = __builtin_amdgcn_raw_buffer_load_b32(rws, WS_E, 0, AUX_SC1);
    const rsrc_t rx = mk(x + (w & 3) * D, w < 4 ? D * 2 : 0);
    unsigned xr[5];
#pragma unroll
    for (int i = 0; i < 5; ++i) xr[i] = __builtin_amdgcn_raw_buffer_load_b32(rx, (lane + 64 * i) * 4, 0, 0);
    const rsrc_t rq = mk(wq, D * H * DH * 2);
    const int c3 = lane >> 4, rs = lane & 15;
    const int qoff = c3 < 3 ? rs * (H * DH * 2) + h * ROWB + js * 48 + c3 * 16 : OOB;
    v4u Wq[5];
#pragma unroll
    for (int i = 0; i < 5; ++i) Wq[i] = ld128<W_AUX>(rq, qoff, (80 * w + 16 * i) * (H * DH * 2));
    SB();
    if (PRO_BAR) __builtin_amdgcn_s_barrier();
    SB();
#pragma unroll
    for (int t = 0; t < NPRE; ++t) { LDK0(t); SB(); }
    SB();
    STAMP(10);
    epoch = __builtin_amdgcn_readfirstlane(e0) * 0x9E3779B1u + 0x7F4A7C15u;
    LAS float* xs = (LAS float*)(L + L_XS);
    if (w < 4) {
      float v[10]; bool valid[10];
#pragma unroll
      for (int i = 0; i < 5; ++i) { v[2 * i] = bflo(xr[i]); v[2 * i + 1] = bfhi(xr[i]); valid[2 * i] = valid[2 * i + 1] = true; }
      float mb, sb, rs_; ln_stats<10>(v, valid, mb, sb, rs_);
#pragma unroll
      for (int i = 0; i < 5; ++i) {
        xs[(2 * (lane + 64 * i)) * 4 + w] = rbf(fdiv(rbf(v[2 * i] - mb), sb, rs_));
        xs[(2 * (lane + 64 * i) + 1) * 4 + w] = rbf(fdiv(rbf(v[2 * i + 1] - mb), sb, rs_));
      }
    }
    __syncthreads();
    STAMP(11);
    f32x2 qa[16];
#pragma unroll
    for (int i = 0; i < 16; ++i) qa[i] = f32x2{0.f, 0.f};
#pragma unroll
    for (int i = 0; i < 5; ++i) {
      const f32x4 xv = *(const LAS f32x4*)(xs + (80 * w + 16 * i + rs) * 4);
#pragma unroll
      for (int k = 0; k < 4; ++k) {
        const f32x2 wv = {bflo(Wq[i][k]), bfhi(Wq[i][k])};
#pragma unroll
        for (int bb = 0; bb < 4; ++bb) qa[bb * 4 + k] = __builtin_elementwise_fma(f32x2{xv[bb], xv[bb]}, wv, qa[bb * 4 + k]);
      }
    }
    float a16[16], a8[8], a4[4], a2[2];
#pragma unroll
    for (int i = 0; i < 16; ++i) a16[i] = tred<XOR8>(qa[i >> 1][i & 1], qa[8 + (i >> 1)][i & 1], lane & 8);
#pragma unroll
    for (int i = 0; i < 8; ++i) a8[i] = tred<XOR7>(a16[i], a16[i + 8], lane & 4);
#pragma unroll
    for (int i = 0; i < 4; ++i) a4[i] = tred<XOR2>(a8[i], a8[i + 4], lane & 2);
#pragma unroll
    for (int i = 0; i < 2; ++i) a2[i] = tred<XOR1>(a4[i], a4[i + 2], lane & 1);
    LAS float* part = (LAS float*)(L + L_PART);
    if (c3 < 3) *(LAS f32x2*)(part + w * 96 + (rs >> 2) * 24 + c3 * 8 + (rs & 3) * 2) = f32x2{a2[0], a2[1]};
    __syncthreads();
    STAMP(12);
    if (w == 0) {
      if (lane < 56) {
        f32x2 s = {0.f, 0.f};
#pragma unroll
        for (int ww = 0; ww < 8; ++ww) s += *(const LAS f32x2*)(part + ww * 96 + 2 * (lane < 48 ? lane : 0));
        const int bb = lane < 48 ? lane / 12 : (lane - 48) >> 1, pp = lane < 48 ? lane % 12 : 12 + (lane & 1);
        const v2u o2 = {lane < 48 ? packbf(s[0], s[1]) : ((unsigned)__builtin_amdgcn_s_getreg(20 | (3 << 11)) & 1u), epoch};
        __builtin_amdgcn_raw_buffer_store_b64(o2, rws, WS_QG + ((h * 4 + bb) * 8 + js) * 128 + pp * 8, 0, AUX_SC1);
      }
      const int off[1] = {lane < 50 ? WS_QG + (h * 4 + b) * 1024 + (lane < 48 ? (lane / 6) * 32 + lane * 16 : ((lane - 48) * 4 + b) * 128 + 96) : OOB};
      v4u o[1];
      gather<1>(rws, off, lane < 50, epoch, o);
      if (lane < 50) *(LAS v2u*)(L + L_QS + lane * 8) = v2u{o[0][0], o[0][2]};
    }
    SB();
#pragma unroll
    for (int t = NPRE; t < NT; ++t) { LDK0(t); SB(); }
    SB();
    __syncthreads();
  }
  unsigned qf[12];
#pragma unroll
  for (int jj = 0; jj < 3; ++jj) { const v4u q4 = *(const LAS v4u*)(L + L_QS + jj * 128 + sub * 16); qf[4 * jj] = q4[0]; qf[4 * jj + 1] = q4[1]; qf[4 * jj + 2] = q4[2]; qf[4 * jj + 3] = q4[3]; }
  const int n0 = 32 + PSHIFT * ((__builtin_amdgcn_readfirstlane(*(const LAS int*)(L + L_QS + 392)) & 1) - (__builtin_amdgcn_readfirstlane(*(const LAS int*)(L + L_QS + 384)) & 1));
  const int nw = ((sp ? 64 - n0 : n0) - w + 7) >> 3;
  STAMP(1);

  LAS float* pb = (LAS float*)(L + L_P) + w * 320;
  float mx = -INFINITY;
#pragma unroll 1
  for (int it = 0; it < nw; ++it) {
    const bool lastk = it + 1 == nw;
    const rsrc_t rn = mk((lastk ? vc : kc) + kvoff, SK * ROWB);
    const int cur = cw + it * cstep, nb = lastk ? cw : cur + cstep;
    float xx[8];
#pragma unroll
    for (int t = 0; t < 8; ++t) {
      float d0 = 0.f, d1 = 0.f, d2 = 0.f;
#pragma unroll
      for (int k = 0; k < 4; ++k) { d0 = dot2(R[t % NT][0][k], qf[k], d0); d1 = dot2(R[t % NT][1][k], qf[4 + k], d1); d2 = dot2(R[t % NT][2][k], qf[8 + k], d2); }
      xx[t] = (d0 + d1) + d2;
      asm volatile("" : "+v"(xx[t]));
      SB();
#pragma unroll
      for (int jj = 0; jj < 3; ++jj)
        R[t % NT][jj] = (t + NT < 8) ? ld128<KV_AUX>(rk, loff + jj * 128, cur + (t + NT) * TILEB) : ld128<KV_AUX>(rn, loff + jj * 128, nb + (t + NT - 8) * TILEB);
      SB();
    }
#pragma unroll
    for (int i = 0; i < 4; ++i) xx[i] = tred<XOR7>(xx[i], xx[i + 4], lane & 4);
#pragma unroll
    for (int i = 0; i < 2; ++i) xx[i] = tred<XOR2>(xx[i], xx[i + 2], lane & 2);
    xx[0] = tred<XOR1>(xx[0], xx[1], lane & 1);
    const float sc = rbf(rbf(xx[0]) * 0.07216878364870323f);
    mx = fmaxf(mx, sc);
    pb[it * 64 + lane] = sc;
  }
  STAMP(2);
  const float mw = wave_max(mx);
  float lsum = 0.f;
  LDS_FENCE();
#pragma unroll 1
  for (int it = 0; it < nw; ++it) { const float p = __builtin_amdgcn_exp2f((pb[it * 64 + lane] - mw) * LOG2E); lsum += p; pb[it * 64 + lane] = p; }
  lsum = wave_sum(lsum);
  LAS float* ml = (LAS float*)(L + L_ML);
  if (lane == 0) { ml[w] = mw; ml[8 + w] = lsum; }
  LDS_FENCE();
  f32x2 acc[12];
#pragma unroll
  for (int i = 0; i < 12; ++i) acc[i] = f32x2{0.f, 0.f};
#pragma unroll 1
  for (int it = 0; it < nw - 1; ++it) {
    const f32x4 p0 = *(const LAS f32x4*)(pb + it * 64 + g * 8), p1 = *(const LAS f32x4*)(pb + it * 64 + g * 8 + 4);
    const int cur = cw + it * cstep;
#pragma unroll
    for (int t = 0; t < 8; ++t) {
      fma_tile(acc, R[t % NT], t < 4 ? p0[t & 3] : p1[t & 3]);
      SB();
#pragma unroll
      for (int jj = 0; jj < 3; ++jj) R[t % NT][jj] = ld128<KV_AUX>(rv, loff + jj * 128, (t + NT < 8) ? cur + (t + NT) * TILEB : cur + cstep + (t + NT - 8) * TILEB);
      SB();
    }
  }
  __syncthreads();
  float M, Lsum, fw;
  {
    const f32x4 m0 = *(const LAS f32x4*)(ml), m1 = *(const LAS f32x4*)(ml + 4), l0 = *(const LAS f32x4*)(ml + 8), l1 = *(const LAS f32x4*)(ml + 12);
    M = fmaxf(fmaxf(fmaxf(m0[0], m0[1]), fmaxf(m0[2], m0[3])), fmaxf(fmaxf(m1[0], m1[1]), fmaxf(m1[2], m1[3])));
    Lsum = 0.f;
#pragma unroll
    for (int i = 0; i < 4; ++i) Lsum = fmaf(__builtin_amdgcn_exp2f((m0[i] - M) * LOG2E), l0[i], Lsum);
#pragma unroll
    for (int i = 0; i < 4; ++i) Lsum = fmaf(__builtin_amdgcn_exp2f((m1[i] - M) * LOG2E), l1[i], Lsum);
    fw = __builtin_amdgcn_exp2f((mw - M) * LOG2E);
  }
  v4u wt[10];
  {
    const rsrc_t ro = mk(wo + (size_t)h * DH * D, isW ? DH * D * 2 : 0);
    const int it = nw - 1, cur = cw + it * cstep;
    const f32x4 p0 = *(const LAS f32x4*)(pb + it * 64 + g * 8), p1 = *(const LAS f32x4*)(pb + it * 64 + g * 8 + 4);
#pragma unroll
    for (int t = 0; t < 8; ++t) {
      fma_tile(acc, R[t % NT], t < 4 ? p0[t & 3] : p1[t & 3]);
      SB();
      if (t + NT < 8) {
#pragma unroll
        for (int jj = 0; jj < 3; ++jj) R[t % NT][jj] = ld128<KV_AUX>(rv, loff + jj * 128, cur + (t + NT) * TILEB);
      } else {
#pragma unroll
        for (int n = (t + NT - 8) * 6 / NT; n < (t + NT - 7) * 6 / NT; ++n) wt[n] = ld128<W_AUX>(ro, (tid >> 4) * (D * 2) + ws5 * 256 + (tid & 15) * 16, n * 32 * D * 2);
      }
      SB();
    }
  }
  STAMP(3);
  {
    float t12[12], u6[6];
#pragma unroll
    for (int i = 0; i < 12; ++i) t12[i] = red32(acc[i >> 1][i & 1], acc[6 + (i >> 1)][i & 1]);
#pragma unroll
    for (int i = 0; i < 6; ++i) u6[i] = red16(t12[i], t12[i + 6]);
    LAS float* part = (LAS float*)(L + L_PART);
    const int ib = 3 * ((lane >> 3) & 1) + 6 * ((lane >> 4) & 1) + 12 * (lane >> 5);
#pragma unroll
    for (int i = 0; i < 3; ++i) { const int idx = ib + i, d = (idx >> 3) * 64 + sub * 8 + (idx & 7); part[d * 8 + w] = tred<XOR8>(u6[i], u6[i + 3], lane & 8) * fw; }
    __syncthreads();
    STAMP(14);
    if (tid < 96) {
      const LAS f32x4* pp = (const LAS f32x4*)(part + tid * 16);
      const f32x4 a0 = pp[0], a1 = pp[1], a2 = pp[2], a3 = pp[3];
      const float s0 = ((a0[0] + a0[1]) + (a0[2] + a0[3])) + ((a1[0] + a1[1]) + (a1[2] + a1[3]));
      const float s1 = ((a2[0] + a2[1]) + (a2[2] + a2[3])) + ((a3[0] + a3[1]) + (a3[2] + a3[3]));
      const v4u o = {__float_as_uint(s0), epoch, __float_as_uint(s1), epoch};
      __builtin_amdgcn_raw_buffer_store_b128(o, rws, WS_OG + ((h * 8 + j) * 104 + tid) * 16, 0, AUX_SC1);
    } else if (tid == 96) {
      const v4u o = {__float_as_uint(M), epoch, __float_as_uint(Lsum), epoch};
      __builtin_amdgcn_raw_buffer_store_b128(o, rws, WS_OG + ((h * 8 + j) * 104 + 96) * 16, 0, AUX_SC1);
    }
  }
  const int ar = lane & 3, ak = (lane >> 4) * 16;
  const int trow = 8 * (lane >> 4) + ((lane & 15) >> 2), tp = (lane & 3) * 8;
  LAS float* red = (LAS float*)(L + L_RED);
  v4u Br[10];
  if (isW) {
#pragma unroll
    for (int n = 0; n < 6; ++n) ((LAS v4u*)(L + L_WR))[n * 512 + tid] = wt[n];
    __syncthreads();
    {
      const LAS unsigned char* po = L + L_WR + trow * 256 + w * 32 + tp;
#pragma unroll
      for (int i = 0; i < 6; ++i) { Br[i] = bfrag(po + i * 32 * 256, 256); asm volatile("" : "+v"(Br[i])); }
    }
    if (w < 6) {
      const int bb = tid / 96, u = tid - bb * 96;
      const int base = WS_OG + (h * 8 + 2 * bb) * 104 * 16;
      const int off[4] = {base + u * 16, base + (104 + u) * 16, base + 96 * 16, base + (104 + 96) * 16};
      v4u o[4];
      gather<4>(rws, off, true, epoch, o);
      const float m1 = __uint_as_float(o[2][0]), m2 = __uint_as_float(o[3][0]), MM = fmaxf(m1, m2);
      const float e1 = __builtin_amdgcn_exp2f((m1 - MM) * LOG2E), e2 = __builtin_amdgcn_exp2f((m2 - MM) * LOG2E);
      const float inv = __builtin_amdgcn_rcpf(__uint_as_float(o[2][2]) * e1 + __uint_as_float(o[3][2]) * e2);
      const float w1 = e1 * inv, w2 = e2 * inv;
      *(LAS unsigned*)(L + L_OS + bb * OS_STR + u * 4) = packbf(w1 * __uint_as_float(o[0][0]) + w2 * __uint_as_float(o[1][0]), w1 * __uint_as_float(o[0][2]) + w2 * __uint_as_float(o[1][2]));
    }
    __syncthreads();
    STAMP(15);
    f32x4 c = {0.f, 0.f, 0.f, 0.f}, c2 = {0.f, 0.f, 0.f, 0.f};
#pragma unroll
    for (int i = 0; i < 6; i += 2) {
      c = mfma(*(const LAS v4u*)(L + L_OS + ar * OS_STR + i * 64 + ak), Br[i], c);
      c2 = mfma(*(const LAS v4u*)(L + L_OS + ar * OS_STR + i * 64 + 64 + ak), Br[i + 1], c2);
    }
    c += c2;
    const v2u o2 = {__float_as_uint(spread4(c)), epoch};
    __builtin_amdgcn_raw_buffer_store_b64(o2, rws, WS_P2 + (((lane >> 4) * 40 + 8 * ws5 + w) * 32 + h) * 128 + (lane & 15) * 8, 0, AUX_SC1);
    STAMP(4);
  } else {
    const rsrc_t rr = mk(isF2 ? wd : (sp ? wg : wu), (isF1 || isF2) ? D * F * 2 : 0);
    const int rvoff = isF1 ? (tid >> 3) * (F * 2) + f0 * 2 + (tid & 7) * 16 : (tid >> 1) * (D * 2) + c0 * 2 + (tid & 1) * 16;
    const int rstep = isF1 ? 64 * F * 2 : 256 * D * 2;
#pragma unroll
    for (int n = 0; n < 10; ++n) wt[n] = ld128<W_AUX>(rr, rvoff, n * rstep);
    SB();
#pragma unroll
    for (int n = 0; n < 10; ++n) ((LAS v4u*)(L + L_WR))[n * 512 + tid] = wt[n];
    __syncthreads();
    const int rs_ = isF1 ? 128 : 32;
    const LAS unsigned char* pq = L + L_WR + (isF1 ? ((w >> 2) * 320 + trow) * 128 + (w & 3) * 32 : (w * 192 + trow) * 32) + tp;
#pragma unroll
    for (int i = 0; i < 10; ++i) { Br[i] = bfrag(pq + (isF1 || i < 6 ? i : 0) * 32 * rs_, rs_); asm volatile("" : "+v"(Br[i])); }
    STAMP(13);
  }

  if (isR) {
    const int r = xl * 10 + (ri >> 2) * 2 + b, rb = r / 20, cb = r - rb * 20;
    unsigned xv = 0;
    if (tid < 16) xv = *(const GAS unsigned*)(x + rb * D + cb * 32 + 2 * tid);
    const int off[1] = {WS_P2 + (rb * 40 + 2 * cb) * 4096 + tid * 16};
    v4u o[1];
    gather<1>(rws, off, true, epoch, o);
    float v = red32(__uint_as_float(o[0][0]), __uint_as_float(o[0][2]));
    v = red16(v, v); v += dpp<XOR8>(v);
    if ((lane & 24) == 0) red[w * 16 + (lane & 7) * 2 + (lane >> 5)] = v;
    __syncthreads();
    if (tid < 16) {
      const LAS f32x2* rp = (const LAS f32x2*)(red + (tid >> 3) * 64 + (tid & 7) * 2);
      const f32x2 s = (rp[0] + rp[8]) + (rp[16] + rp[24]);
      const v2u o2 = {packbf(bflo(xv) + rbf(s[0]), bfhi(xv) + rbf(s[1])), epoch};
      __builtin_amdgcn_raw_buffer_store_b64(o2, rws, WS_HG + (rb * 320 + cb * 16 + tid) * 8, 0, AUX_SC1);
    }
    STAMP(5);
  } else if (isF1) {
    if (w < 4) {
      const bool a2 = lane < 32;
      const int off[3] = {WS_HG + (w * 160 + lane) * 16, WS_HG + (w * 160 + 64 + lane) * 16, WS_HG + (w * 160 + 128 + (lane & 31)) * 16};
      v4u o[3];
      gather<3>(rws, off, true, epoch, o);
      float v[12]; bool valid[12];
#pragma unroll
      for (int k = 0; k < 3; ++k) {
        const bool a = k < 2 || a2;
        v[4 * k] = a ? bflo(o[k][0]) : 0.f; v[4 * k + 1] = a ? bfhi(o[k][0]) : 0.f; v[4 * k + 2] = a ? bflo(o[k][2]) : 0.f; v[4 * k + 3] = a ? bfhi(o[k][2]) : 0.f;
        valid[4 * k] = valid[4 * k + 1] = valid[4 * k + 2] = valid[4 * k + 3] = a;
      }
      float mb, sb, rs_; ln_stats<12>(v, valid, mb, sb, rs_);
#pragma unroll
      for (int k = 0; k < 3; ++k) if (k < 2 || a2)
        *(LAS v2u*)(L + L_ACT + w * X2_STR + (64 * k + lane) * 8) =
            v2u{packbf(fdiv(rbf(v[4 * k] - mb), sb, rs_), fdiv(rbf(v[4 * k + 1] - mb), sb, rs_)), packbf(fdiv(rbf(v[4 * k + 2] - mb), sb, rs_), fdiv(rbf(v[4 * k + 3] - mb), sb, rs_))};
    }
    __syncthreads();
    STAMP(6);
    f32x4 c = {0.f, 0.f, 0.f, 0.f}, c2 = {0.f, 0.f, 0.f, 0.f};
#pragma unroll
    for (int i = 0; i < 10; i += 2) {
      c = mfma(*(const LAS v4u*)(L + L_ACT + ar * X2_STR + (w >> 2) * 640 + i * 64 + ak), Br[i], c);
      c2 = mfma(*(const LAS v4u*)(L + L_ACT + ar * X2_STR + (w >> 2) * 640 + i * 64 + 64 + ak), Br[i + 1], c2);
    }
    const float half = spread4(c + c2);
    if (w >= 4) red[(w & 3) * 64 + lane] = half;
    __syncthreads();
    if (w < 4) {
      const float z = rbf(half + red[w * 64 + lane]);
      const float dn = 1.0f + __expf(-z);
      const float v = sp ? fdiv(z, dn, __builtin_amdgcn_rcpf(dn)) : z;
      const float nb = dpp<XOR1>(v);
      if (!(lane & 1)) {
        const v2u o2 = {packbf(v, nb), epoch};
        __builtin_amdgcn_raw_buffer_store_b64(o2, rws, WS_AG + (sp ? 0 : 96 * 256) + f0 * 16 + w * 256 + lane * 4, 0, AUX_SC1);
      }
    }
    STAMP(7);
  } else if (isF2) {
    unsigned hp = 0;
    if (w == 0) {
      const int offh[1] = {WS_HG + (lane >> 4) * 2560 + ((c0 + (lane & 15)) >> 2) * 16};
      v4u oh[1];
      gather<1>(rws, offh, true, epoch, oh);
      hp = (lane & 2) ? oh[0][2] : oh[0][0];
    }
    const int off[6] = {WS_AG + tid * 16, WS_AG + (512 + tid) * 16, WS_AG + (1024 + tid) * 16, WS_AG + (1536 + tid) * 16, WS_AG + (2048 + tid) * 16, WS_AG + (2560 + tid) * 16};
    v4u o[6];
    gather<6>(rws, off, true, epoch, o);
#pragma unroll
    for (int k = 0; k < 3; ++k)
      *(LAS v2u*)(L + L_ACT + ((tid >> 2) & 3) * AS_STR + ((512 * k + tid) >> 4) * 32 + (tid & 3) * 8) =
          v2u{packbf(bflo(o[k][0]) * bflo(o[k + 3][0]), bfhi(o[k][0]) * bfhi(o[k + 3][0])), packbf(bflo(o[k][2]) * bflo(o[k + 3][2]), bfhi(o[k][2]) * bfhi(o[k + 3][2]))};
    __syncthreads();
    STAMP(8);
    f32x4 c = {0.f, 0.f, 0.f, 0.f}, c2 = {0.f, 0.f, 0.f, 0.f};
#pragma unroll
    for (int i = 0; i < 6; i += 2) {
      c = mfma(*(const LAS v4u*)(L + L_ACT + ar * AS_STR + w * 384 + i * 64 + ak), Br[i], c);
      c2 = mfma(*(const LAS v4u*)(L + L_ACT + ar * AS_STR + w * 384 + i * 64 + 64 + ak), Br[i + 1], c2);
    }
    red[w * 64 + lane] = spread4(c + c2);
    __syncthreads();
    if (w == 0) {
      const float s = ((red[lane] + red[64 + lane]) + (red[128 + lane] + red[192 + lane])) + ((red[256 + lane] + red[320 + lane]) + (red[384 + lane] + red[448 + lane]));
      const float hv = (lane & 1) ? bfhi(hp) : bflo(hp);
      out[(lane >> 4) * D + c0 + (lane & 15)] = (u16)bf16bits(hv + rbf(s));
      if (c0 == 0 && lane == 0) __builtin_amdgcn_raw_buffer_store_b32(epoch, rws, WS_E, 0, AUX_SC1);
    }
    STAMP(9);
  }
  STAMP_FLUSH();
}

extern "C" void kernel_launch(void* const* d_in, const int* in_sizes, int n_in, void* d_out, int out_size, void* d_ws, size_t ws_size,
                              hipStream_t stream) {
  block_kernel<<<NWG, NTHR, 0, stream>>>((const u16*)d_in[0], (const u16*)d_in[1], (const u16*)d_in[2], (const u16*)d_in[3], (const u16*)d_in[4],
                                         (const u16*)d_in[6], (const u16*)d_in[7], (const u16*)d_in[8], (u16*)d_out, (unsigned char*)d_ws);
}
```

```cpp
#include <hip/hip_runtime.h>
#include <stdint.h>

typedef unsigned v4u __attribute__((ext_vector_type(4)));
typedef unsigned v2u __attribute__((ext_vector_type(2)));
typedef float f32x4 __attribute__((ext_vector_type(4)));
typedef float f32x2 __attribute__((ext_vector_type(2)));
typedef __bf16 bf16x2 __attribute__((ext_vector_type(2)));
typedef unsigned short u16;
typedef short s16x4 __attribute__((ext_vector_type(4)));
typedef __bf16 bf16x8 __attribute__((ext_vector_type(8)));
#define GAS __attribute__((address_space(1)))
#define LAS __attribute__((address_space(3)))
typedef __amdgpu_buffer_rsrc_t rsrc_t;

constexpr int D = 640, H = 32, SK = 4096, DH = 192, F = 1536;
constexpr int NWG = 256, NTHR = 512;
constexpr int KV_AUX = 2, W_AUX = 2;
constexpr int NPRE0 = 1, NPRE = 4;
constexpr int PSHIFT = 2;
constexpr int ROWB = DH * 2  , TILEB = 8 * ROWB  , CHB = 8 * TILEB  ;
constexpr int AUX_SC1 = 16, RSRC_W3 = 0x00020000, OOB = 0x7ffffff0;
constexpr unsigned SPIN_CAP = 20000u;
constexpr float LOG2E = 1.4426950408889634f;
constexpr int WS_E = 0;
constexpr int WS_QP = 4096;
constexpr int WS_QG = WS_QP + 128 * 2 * 128;
constexpr int WS_OG = WS_QG + 32 * 12 * 256;
constexpr int WS_P2 = WS_OG + 32 * 8 * 104 * 16;
constexpr int WS_HG = WS_P2 + 4 * 40 * 32 * 128;
constexpr int WS_AG = WS_HG + 4 * 320 * 8;
constexpr int WS_END = WS_AG + 2 * 96 * 256;
constexpr int OS_STR = 400, X2_STR = 1296, AS_STR = 3088;
constexpr int L_PART = 0;
constexpr int L_QS = L_PART + 8 * 200 * 4;
constexpr int L_P = L_QS + 192 * 2 + 16;
constexpr int L_ML = L_P + 8 * 320 * 4;
constexpr int L_OS = L_ML + 80;
constexpr int L_RED = L_OS + 4 * OS_STR;
constexpr int L_ACT = L_RED + 8 * 64 * 4;
constexpr int L_WR = L_ACT + 4 * AS_STR;
constexpr int L_END = L_WR + 2 * 640 * 64;
static_assert(L_END <= 160 * 1024, "LDS");

__device__ __forceinline__ float dot2(unsigned a, unsigned b, float c) {
  return __builtin_amdgcn_fdot2_f32_bf16(__builtin_bit_cast(bf16x2, a), __builtin_bit_cast(bf16x2, b), c, false);
}
__device__ __forceinline__ float bflo(unsigned u) { return __builtin_bit_cast(float, u << 16); }
__device__ __forceinline__ float bfhi(unsigned u) { return __builtin_bit_cast(float, u & 0xffff0000u); }
__device__ __forceinline__ unsigned bf16bits(float f) { return (unsigned)__builtin_bit_cast(u16, (__bf16)f); }
__device__ __forceinline__ float rbf(float f) { return (float)(__bf16)f; }
__device__ __forceinline__ unsigned packbf(float lo, float hi) {
  bf16x2 v = {(__bf16)lo, (__bf16)hi}; return __builtin_bit_cast(unsigned, v);
}
template <int CTRL> __device__ __forceinline__ float dpp(float x) {
  return __builtin_bit_cast(float, __builtin_amdgcn_mov_dpp(__builtin_bit_cast(int, x), CTRL, 0xf, 0xf, true));
}
constexpr int XOR1 = 0xB1, XOR2 = 0x4E, XOR7 = 0x141, XOR8 = 0x128;
__device__ __forceinline__ float red32(float a, float b) {
  auto r = __builtin_amdgcn_permlane32_swap(__float_as_uint(a), __float_as_uint(b), false, false);
  return __uint_as_float(r[0]) + __uint_as_float(r[1]);
}
__device__ __forceinline__ float red16(float a, float b) {
  auto r = __builtin_amdgcn_permlane16_swap(__float_as_uint(a), __float_as_uint(b), false, false);
  return __uint_as_float(r[0]) + __uint_as_float(r[1]);
}
template <int CTRL> __device__ __forceinline__ float tred(float lo, float hi, bool sel) {
  const float keep = sel ? hi : lo, send = sel ? lo : hi; return keep + dpp<CTRL>(send);
}
__device__ __forceinline__ float wave_sum(float x) {
  x += dpp<XOR1>(x); x += dpp<XOR2>(x); x += dpp<XOR7>(x); x += dpp<XOR8>(x);
  x = red16(x, x); x = red32(x, x); return x;
}
__device__ __forceinline__ float wave_max(float x) {
  x = fmaxf(x, dpp<XOR1>(x)); x = fmaxf(x, dpp<XOR2>(x)); x = fmaxf(x, dpp<XOR7>(x)); x = fmaxf(x, dpp<XOR8>(x));
  { auto r = __builtin_amdgcn_permlane16_swap(__float_as_uint(x), __float_as_uint(x), false, false); x = fmaxf(__uint_as_float(r[0]), __uint_as_float(r[1])); }
  { auto r = __builtin_amdgcn_permlane32_swap(__float_as_uint(x), __float_as_uint(x), false, false); x = fmaxf(__uint_as_float(r[0]), __uint_as_float(r[1])); }
  return x;
}
__device__ __forceinline__ rsrc_t mk(const void* p, unsigned bytes) { return __builtin_amdgcn_make_buffer_rsrc((void*)p, 0, (int)bytes, RSRC_W3); }
template <int AUX> __device__ __forceinline__ v4u ld128(rsrc_t r, int voff, int soff) { return __builtin_amdgcn_raw_buffer_load_b128(r, voff, soff, AUX); }
__device__ __forceinline__ f32x4 mfma(v4u a, v4u b, f32x4 c) {
  return __builtin_amdgcn_mfma_f32_16x16x32_bf16(__builtin_bit_cast(bf16x8, a), __builtin_bit_cast(bf16x8, b), c, 0, 0, 0);
}
__device__ __forceinline__ v4u bfrag(const LAS unsigned char* p, int stride) {
  const v2u lo = __builtin_bit_cast(v2u, __builtin_amdgcn_ds_read_tr16_b64_v4i16((LAS s16x4*)p));
  const v2u hi = __builtin_bit_cast(v2u, __builtin_amdgcn_ds_read_tr16_b64_v4i16((LAS s16x4*)(p + 4 * stride)));
  return v4u{lo[0], lo[1], hi[0], hi[1]};
}
__device__ __forceinline__ float spread4(f32x4 c) {
  const auto a = __builtin_amdgcn_permlane16_swap(__float_as_uint(c[0]), __float_as_uint(c[1]), false, false);
  const auto d = __builtin_amdgcn_permlane16_swap(__float_as_uint(c[2]), __float_as_uint(c[3]), false, false);
  const auto r = __builtin_amdgcn_permlane32_swap(a[0], d[0], false, false);
  return __uint_as_float(r[0]);
}
#define SB() __builtin_amdgcn_sched_barrier(0)
#define PIN(x) asm volatile("" : "+v"(x))
#define LDS_FENCE() asm volatile("s_waitcnt lgkmcnt(0)" ::: "memory")

__device__ __forceinline__ void fma_tile(f32x2 (&acc)[12], const v4u (&t)[3], float p) {
  const f32x2 pp = {p, p};
#pragma unroll
  for (int j = 0; j < 3; ++j)
#pragma unroll
    for (int k = 0; k < 4; ++k) { const f32x2 v = {bflo(t[j][k]), bfhi(t[j][k])}; acc[j * 4 + k] = __builtin_elementwise_fma(pp, v, acc[j * 4 + k]); }
  asm volatile("" : "+v"(acc[0]), "+v"(acc[1]), "+v"(acc[2]), "+v"(acc[3]), "+v"(acc[4]), "+v"(acc[5]), "+v"(acc[6]), "+v"(acc[7]), "+v"(acc[8]), "+v"(acc[9]), "+v"(acc[10]), "+v"(acc[11]));
}
template <int N> __device__ __forceinline__ void ln_stats(const float (&v)[N], const bool (&valid)[N], float& mb, float& sb, float& rs) {
  const float x0 = __builtin_bit_cast(float, __builtin_amdgcn_readfirstlane(__builtin_bit_cast(int, v[0])));
  float s1 = 0.f, s2 = 0.f;
#pragma unroll
  for (int i = 0; i < N; ++i) { const float d = valid[i] ? v[i] - x0 : 0.f; s1 += d; s2 = fmaf(d, d, s2); }
  const float m1 = wave_sum(s1) * (1.0f / D), m2 = wave_sum(s2) * (1.0f / D);
  mb = rbf(x0 + m1);
  sb = rbf(__builtin_amdgcn_sqrtf(rbf(rbf(fmaxf(fmaf(-m1, m1, m2), 0.f)) + 1e-5f)));
  rs = __builtin_amdgcn_rcpf(sb);
}
__device__ __forceinline__ float fdiv(float a, float b, float rb) { const float q = a * rb; return fmaf(fmaf(-q, b, a), rb, q); }

template <int N> __device__ __forceinline__ void gather(rsrc_t rs, const int (&off)[N], bool act, unsigned epoch, v4u (&o)[N]) {
  for (unsigned spins = 0;; ++spins) {
    asm volatile("" ::: "memory");
    bool ok = true;
#pragma unroll
    for (int k = 0; k < N; ++k) o[k] = ld128<AUX_SC1>(rs, off[k], 0);
#pragma unroll
    for (int k = 0; k < N; ++k) ok = ok & (o[k][1] == epoch) & (o[k][3] == epoch);
    if (__all(ok | !act) || spins > SPIN_CAP) break;
    __builtin_amdgcn_s_sleep(1);
  }
  asm volatile("" ::: "memory");
}

__global__ void __launch_bounds__(NTHR, 2)
block_kernel(const u16* __restrict__ x, const u16* __restrict__ kc, const u16* __restrict__ vc, const u16* __restrict__ wq,
             const u16* __restrict__ wo, const u16* __restrict__ wg, const u16* __restrict__ wu, const u16* __restrict__ wd,
             u16* __restrict__ out, unsigned char* __restrict__ ws) {
  __shared__ __attribute__((aligned(16))) unsigned char smem[L_END];
  LAS unsigned char* L = (LAS unsigned char*)smem;
  const int bid = blockIdx.x, tid = threadIdx.x, lane = tid & 63, w = __builtin_amdgcn_readfirstlane(tid >> 6);
  const rsrc_t rws = mk(ws, WS_END);
  const int ri = bid >> 3, xl = bid & 7, sp = xl & 1;
  const int unit = (((sp ? xl + 5 : xl) & 7) >> 1) * 32 + ri, h = unit >> 2, b = unit & 3;
  const int j = 2 * b + sp;
  const bool isW = !sp || b == 0;
  const int g = lane >> 3, sub = lane & 7;
  const size_t kvoff = (size_t)(b * H + h) * SK * DH;
  const rsrc_t rk = mk(kc + kvoff, SK * ROWB), rv = mk(vc + kvoff, SK * ROWB);
#define CHK(li) ((sp ? ((li) < 32 - PSHIFT ? 32 + PSHIFT + (li) : 63 - (li)) : (li)) * CHB)
#define TST(i) (((i) < 3) ? (sp ? (32 + PSHIFT) * CHB : 0) + (64 * (i) + w) * TILEB : CHK(w + 8 * (i)))
#define TSD(i) (((i) < 3) ? 8 * TILEB : TILEB)
  const int loff = g * ROWB + sub * 16, cw = TST(0);
  asm volatile("" :: "s"(x), "s"(kc), "s"(vc), "s"(wq), "s"(wo), "s"(wg), "s"(wu), "s"(wd), "s"(out), "s"(ws));
  v4u X[8][3], R[8][3];
#define LDK0(t) _Pragma("unroll") for (int jj = 0; jj < 3; ++jj) X[t][jj] = ld128<KV_AUX>(rk, loff + jj * 128, cw + (t) * TSD(0))
#define LDK1(t) _Pragma("unroll") for (int jj = 0; jj < 3; ++jj) R[t][jj] = ld128<KV_AUX>(rk, loff + jj * 128, TST(1) + (t) * TSD(1))

  unsigned tv = 0;
  unsigned epoch;
  const int ar = lane & 3, ak = (lane >> 4) * 16;
  const int trow = 8 * (lane >> 4) + ((lane & 15) >> 2), tp = (lane & 3) * 8;
  {
#pragma unroll
    for (int t = 0; t < NPRE0; ++t) { LDK0(t); SB(); }
    const unsigned e0 = __builtin_amdgcn_raw_buffer_load_b32(rws, WS_E, 0, AUX_SC1);
    const rsrc_t rx = mk(x + (w & 3) * D, (w < 4 && !isW) ? D * 2 : 0);
    unsigned xr[5];
#pragma unroll
    for (int i = 0; i < 5; ++i) xr[i] = __builtin_amdgcn_raw_buffer_load_b32(rx, (lane + 64 * i) * 4, 0, 0);
    const int q3 = b - 1;
    const rsrc_t rq = mk(wq + h * DH + q3 * 64, isW ? 0 : (D - 1) * H * DH * 2 + 128);
    v4u Wq[10];
#pragma unroll
    for (int n = 0; n < 10; ++n) Wq[n] = ld128<W_AUX>(rq, (tid >> 3) * (H * DH * 2) + (tid & 7) * 16, n * 64 * H * DH * 2);
    SB();
#pragma unroll
    for (int t = NPRE0; t < NPRE; ++t) { LDK0(t); SB(); }
    SB();
    epoch = __builtin_amdgcn_readfirstlane(e0) * 0x9E3779B1u + 0x7F4A7C15u;
    if (tid == 256) *(LAS int*)(L + L_ML + 64) = 0;
    {
      const v2u o2 = {(unsigned)__builtin_amdgcn_s_getreg(20 | (3 << 11)) & 1u, epoch};
      __builtin_amdgcn_raw_buffer_store_b64(o2, rws, tid < 2 ? WS_QP + (unit * 2 + sp) * 128 + tid * 8 : OOB, 0, AUX_SC1);
    }
    if (!isW) {
      if (w < 4) {
        float v[10]; bool valid[10];
#pragma unroll
        for (int i = 0; i < 5; ++i) { v[2 * i] = bflo(xr[i]); v[2 * i + 1] = bfhi(xr[i]); valid[2 * i] = valid[2 * i + 1] = true; }
        float mb, sb, rs_; ln_stats<10>(v, valid, mb, sb, rs_);
#pragma unroll
        for (int i = 0; i < 5; ++i)
          *(LAS unsigned*)(L + L_ACT + w * X2_STR + (lane + 64 * i) * 4) = packbf(fdiv(rbf(v[2 * i] - mb), sb, rs_), fdiv(rbf(v[2 * i + 1] - mb), sb, rs_));
      }
#pragma unroll
      for (int n = 0; n < 10; ++n) ((LAS v4u*)(L + L_WR))[n * 512 + tid] = Wq[n];
      __syncthreads();
      const LAS unsigned char* pq = L + L_WR + ((w >> 2) * 320 + trow) * 128 + (w & 3) * 32 + tp;
      const LAS unsigned char* pa_ = L + L_ACT + ar * X2_STR + (w >> 2) * 640 + ak;
      f32x4 c = {0.f, 0.f, 0.f, 0.f}, c2 = {0.f, 0.f, 0.f, 0.f};
#pragma unroll
      for (int i = 0; i < 10; i += 2) {
        c = mfma(*(const LAS v4u*)(pa_ + i * 64), bfrag(pq + i * 4096, 128), c);
        c2 = mfma(*(const LAS v4u*)(pa_ + i * 64 + 64), bfrag(pq + i * 4096 + 4096, 128), c2);
      }
      const float half = spread4(c + c2);
      LAS float* rp = (LAS float*)(L + L_RED) + (w & 3) * 64 + lane;
      if (w >= 4) *rp = half;
      __syncthreads();
      if (w < 4) {
        const float z = half + *rp;
        const v2u o2 = {packbf(z, dpp<XOR1>(z)), epoch};
        __builtin_amdgcn_raw_buffer_store_b64(o2, rws, (lane & 1) ? OOB : WS_QG + (h * 12 + q3 * 4 + w) * 256 + lane * 4, 0, AUX_SC1);
      }
    }
    SB();
#pragma unroll
    for (int t = NPRE; t < 8; ++t) { LDK0(t); SB(); }
    if (w == 0) {
      const int off[1] = {lane < 48 ? WS_QG + (h * 12 + (lane >> 2)) * 256 + b * 64 + (lane & 3) * 16 : WS_QP + (unit * 2 + (lane & 1)) * 128};
      v4u o[1];
      gather<1>(rws, off, true, epoch, o);
      if (lane < 50) *(LAS v2u*)(L + L_QS + lane * 8) = v2u{o[0][0], o[0][2]};
    }
    SB();
#pragma unroll
    for (int t = 0; t < 8; ++t) { LDK1(t); SB(); }
    SB();
    if (w == 7) {
      const int rr_ = (ri >> 2) * 3 + b - 1;
      const char* wb = isW ? (const char*)(wo + (size_t)h * DH * D) : (const char*)(rr_ < 10 ? wd : (rr_ < 16 ? wg : wu));
      const int wspan = isW ? DH * D * 2 : D * F * 2, k8 = lane & 7;
      const int kvo = sp ? SK * ROWB - 4 - k8 * (SK * ROWB / 14 & ~3) : k8 * (SK * ROWB / 14 & ~3);
      const char* pp = lane < 8 ? (const char*)(vc + kvoff) + kvo : (lane < 16 ? (const char*)(kc + kvoff) + kvo : (lane < 24 ? wb + k8 * ((wspan - 4) / 7 & ~3) : (const char*)out));
      if (lane < 25) tv = __builtin_nontemporal_load((const GAS unsigned*)pp);
    }
    __syncthreads();
  }
  const bool isR = !sp && ri < 20;
  const int ws5 = sp ? 4 : b;
  const int rk_ = (ri >> 2) * 3 + b - 1;
  const bool isF2 = !isW && rk_ < 10, isF1 = !isW && rk_ >= 10 && rk_ < 22, isG = rk_ < 16;
  const int f0 = 64 * ((xl >> 1) * 6 + rk_ - (isG ? 10 : 16)), c0 = 16 * ((xl >> 1) * 10 + rk_);
  unsigned qf[12];
#pragma unroll
  for (int jj = 0; jj < 3; ++jj) { const v4u q4 = *(const LAS v4u*)(L + L_QS + jj * 128 + sub * 16); qf[4 * jj] = q4[0]; qf[4 * jj + 1] = q4[1]; qf[4 * jj + 2] = q4[2]; qf[4 * jj + 3] = q4[3]; }
  const int dpar = (__builtin_amdgcn_readfirstlane(*(const LAS int*)(L + L_QS + 392)) & 1) - (__builtin_amdgcn_readfirstlane(*(const LAS int*)(L + L_QS + 384)) & 1);
    const int n0 = 32 + dpar + (b == 0 ? 0 : 1);
  const int nw = ((sp ? 64 - n0 : n0) - w + 7) >> 3;

  LAS float* pb = (LAS float*)(L + L_P) + w * 320;
  float mx = -INFINITY;
#define SCORES(it) do { \
    _Pragma("unroll") for (int i = 0; i < 4; ++i) xx[i] = tred<XOR7>(xx[i], xx[i + 4], lane & 4); \
    _Pragma("unroll") for (int i = 0; i < 2; ++i) xx[i] = tred<XOR2>(xx[i], xx[i + 2], lane & 2); \
    xx[0] = tred<XOR1>(xx[0], xx[1], lane & 1); \
    const float sc = rbf(rbf(xx[0]) * 0.07216878364870323f);       \
    mx = fmaxf(mx, sc); pb[(it) * 64 + lane] = sc; } while (0)
#define DOT_TILE(KT, OUT) do { float d0_ = 0.f, d1_ = 0.f, d2_ = 0.f; \
    _Pragma("unroll") for (int k_ = 0; k_ < 4; ++k_) { d0_ = dot2(KT[0][k_], qf[k_], d0_); d1_ = dot2(KT[1][k_], qf[4 + k_], d1_); d2_ = dot2(KT[2][k_], qf[8 + k_], d2_); } \
    OUT = (d0_ + d1_) + d2_; asm volatile("" : "+v"(OUT)); SB(); } while (0)
  {
    float xx[8];
#pragma unroll
    for (int t = 0; t < 8; ++t) {
      DOT_TILE(X[t], xx[t]);
    }
    SCORES(0);
  }
#pragma unroll 1
  for (int it = 1; it < nw; ++it) {
    const bool lastk = it + 1 == nw;
    const rsrc_t rn = mk((lastk ? vc : kc) + kvoff, SK * ROWB);
    const int nb = lastk ? cw : TST(it + 1), ns = lastk ? TSD(0) : TSD(it + 1);
    float xx[8];
#pragma unroll
    for (int t = 0; t < 8; ++t) {
      DOT_TILE(R[t], xx[t]);
#pragma unroll
      for (int jj = 0; jj < 3; ++jj)
        R[t][jj] = ld128<KV_AUX>(rn, loff + jj * 128, nb + t * ns);
      SB();
    }
    SCORES(it);
  }
  const float mw = wave_max(mx);
  float lsum = 0.f;
  LDS_FENCE();
#pragma unroll 1
  for (int it = 0; it < nw; ++it) { const float p = __builtin_amdgcn_exp2f((pb[it * 64 + lane] - mw) * LOG2E); lsum += p; pb[it * 64 + lane] = p; }
  lsum = wave_sum(lsum);
  LAS float* ml = (LAS float*)(L + L_ML);
  if (lane == 0) { ml[w] = mw; ml[8 + w] = lsum; LDS_FENCE(); __hip_atomic_fetch_add((LAS int*)(L + L_ML + 64), 1, __ATOMIC_RELAXED, __HIP_MEMORY_SCOPE_WORKGROUP); }
  LDS_FENCE();
  f32x2 acc[12];
#pragma unroll
  for (int i = 0; i < 12; ++i) acc[i] = f32x2{0.f, 0.f};
#pragma unroll 1
  for (int it = 0; it < nw - 1; ++it) {
    const f32x4 p0 = *(const LAS f32x4*)(pb + it * 64 + g * 8), p1 = *(const LAS f32x4*)(pb + it * 64 + g * 8 + 4);
    const int nb = TST(it + 1), ns = TSD(it + 1);
#pragma unroll
    for (int t = 0; t < 8; ++t) {
      fma_tile(acc, R[t], t < 4 ? p0[t & 3] : p1[t & 3]);
      SB();
#pragma unroll
      for (int jj = 0; jj < 3; ++jj) R[t][jj] = ld128<KV_AUX>(rv, loff + jj * 128, nb + t * ns);
      SB();
    }
  }
  while (__builtin_amdgcn_readfirstlane(*(volatile LAS int*)(L + L_ML + 64)) < 8) __builtin_amdgcn_s_sleep(8);
  float M, Lsum, fw;
  {
    const f32x4 m0 = *(const volatile LAS f32x4*)(ml), m1 = *(const volatile LAS f32x4*)(ml + 4), l0 = *(const volatile LAS f32x4*)(ml + 8), l1 = *(const volatile LAS f32x4*)(ml + 12);
    M = fmaxf(fmaxf(fmaxf(m0[0], m0[1]), fmaxf(m0[2], m0[3])), fmaxf(fmaxf(m1[0], m1[1]), fmaxf(m1[2], m1[3])));
    Lsum = 0.f;
#pragma unroll
    for (int i = 0; i < 4; ++i) Lsum = fmaf(__builtin_amdgcn_exp2f((m0[i] - M) * LOG2E), l0[i], Lsum);
#pragma unroll
    for (int i = 0; i < 4; ++i) Lsum = fmaf(__builtin_amdgcn_exp2f((m1[i] - M) * LOG2E), l1[i], Lsum);
    fw = __builtin_amdgcn_exp2f((mw - M) * LOG2E);
    PIN(M); PIN(Lsum); PIN(fw);
  }
  int pa[3], ogo = tid < 97 ? WS_OG + ((h * 8 + j) * 104 + tid) * 16 : OOB;
  {
    const int ib = 3 * ((lane >> 3) & 1) + 6 * ((lane >> 4) & 1) + 12 * (lane >> 5);
#pragma unroll
    for (int i = 0; i < 3; ++i) { const int idx = ib + i; pa[i] = L_PART + (w * 200 + (idx >> 3) * 64 + sub * 8 + (idx & 7)) * 4; PIN(pa[i]); }
    PIN(ogo);
  }
  v4u wt[10];
  {
    const rsrc_t ro = mk(wo + (size_t)h * DH * D, isW ? DH * D * 2 : 0);
    const int it = nw - 1;
    const f32x4 p0 = *(const LAS f32x4*)(pb + it * 64 + g * 8), p1 = *(const LAS f32x4*)(pb + it * 64 + g * 8 + 4);
#pragma unroll
    for (int n = 0; n < 6; ++n) wt[n] = ld128<W_AUX>(ro, (tid >> 4) * (D * 2) + ws5 * 256 + (tid & 15) * 16, n * 32 * D * 2);
    SB();
#pragma unroll
    for (int t = 0; t < 8; ++t) { fma_tile(acc, R[t], t < 4 ? p0[t & 3] : p1[t & 3]); SB(); }
  }
  asm volatile("" :: "v"(tv));
  {
    float t12[12], u6[6];
#pragma unroll
    for (int i = 0; i < 12; ++i) t12[i] = red32(acc[i >> 1][i & 1], acc[6 + (i >> 1)][i & 1]);
#pragma unroll
    for (int i = 0; i < 6; ++i) u6[i] = red16(t12[i], t12[i + 6]);
#pragma unroll
    for (int i = 0; i < 3; ++i) *(LAS float*)(L + pa[i]) = tred<XOR8>(u6[i], u6[i + 3], lane & 8) * fw;
    if (isW) {
#pragma unroll
      for (int n = 0; n < 6; ++n) ((LAS v4u*)(L + L_WR))[n * 512 + tid] = wt[n];
    }
    __syncthreads();
    {
      const LAS f32x2* pp = (const LAS f32x2*)(L + L_PART) + tid;
      const f32x2 s = ((pp[0] + pp[100]) + (pp[200] + pp[300])) + ((pp[400] + pp[500]) + (pp[600] + pp[700]));
      const v4u o = {__float_as_uint(tid < 96 ? s[0] : M), epoch, __float_as_uint(tid < 96 ? s[1] : Lsum), epoch};
      __builtin_amdgcn_raw_buffer_store_b128(o, rws, ogo, 0, AUX_SC1);
    }
  }
  LAS float* red = (LAS float*)(L + L_RED);
  v4u Br[10];
  if (isW) {
    {
      const LAS unsigned char* po = L + L_WR + trow * 256 + w * 32 + tp;
#pragma unroll
      for (int i = 0; i < 6; ++i) { Br[i] = bfrag(po + i * 32 * 256, 256); asm volatile("" : "+v"(Br[i])); }
    }
    int p2o = WS_P2 + (((lane >> 4) * 40 + 8 * ws5 + w) * 32 + h) * 128 + (lane & 15) * 8, aa = L_OS + ar * OS_STR + ak;
    PIN(p2o); PIN(aa);
    if (w < 6) {
      const int bb = tid / 96, u = tid - bb * 96;
      int osa = L_OS + bb * OS_STR + u * 4; PIN(osa);
      const int base = WS_OG + (h * 8 + 2 * bb) * 104 * 16;
      const int off[4] = {base + u * 16, base + (104 + u) * 16, base + 96 * 16, base + (104 + 96) * 16};
      v4u o[4];
      gather<4>(rws, off, true, epoch, o);
      const float m1 = __uint_as_float(o[2][0]), m2 = __uint_as_float(o[3][0]), MM = fmaxf(m1, m2);
      const float e1 = __builtin_amdgcn_exp2f((m1 - MM) * LOG2E), e2 = __builtin_amdgcn_exp2f((m2 - MM) * LOG2E);
      const float inv = __builtin_amdgcn_rcpf(__uint_as_float(o[2][2]) * e1 + __uint_as_float(o[3][2]) * e2);
      const float w1 = e1 * inv, w2 = e2 * inv;
      *(LAS unsigned*)(L + osa) = packbf(w1 * __uint_as_float(o[0][0]) + w2 * __uint_as_float(o[1][0]), w1 * __uint_as_float(o[0][2]) + w2 * __uint_as_float(o[1][2]));
    }
    __syncthreads();
    f32x4 c = {0.f, 0.f, 0.f, 0.f}, c2 = {0.f, 0.f, 0.f, 0.f};
#pragma unroll
    for (int i = 0; i < 6; i += 2) {
      c = mfma(*(const LAS v4u*)(L + aa + i * 64), Br[i], c);
      c2 = mfma(*(const LAS v4u*)(L + aa + i * 64 + 64), Br[i + 1], c2);
    }
    c += c2;
    const v2u o2 = {__float_as_uint(spread4(c)), epoch};
    __builtin_amdgcn_raw_buffer_store_b64(o2, rws, p2o, 0, AUX_SC1);
  } else {
    const rsrc_t rr = mk(isF2 ? wd : (isG ? wg : wu), (isF1 || isF2) ? D * F * 2 : 0);
    const int rvoff = isF1 ? (tid >> 3) * (F * 2) + f0 * 2 + (tid & 7) * 16 : (tid >> 1) * (D * 2) + c0 * 2 + (tid & 1) * 16;
    const int rstep = isF1 ? 64 * F * 2 : 256 * D * 2;
#pragma unroll
    for (int n = 0; n < 10; ++n) wt[n] = ld128<W_AUX>(rr, rvoff, n * rstep);
    SB();
#pragma unroll
    for (int n = 0; n < 10; ++n) ((LAS v4u*)(L + L_WR))[n * 512 + tid] = wt[n];
    __syncthreads();
    const int rs_ = isF1 ? 128 : 32;
    const LAS unsigned char* pq = L + L_WR + (isF1 ? ((w >> 2) * 320 + trow) * 128 + (w & 3) * 32 : (w * 192 + trow) * 32) + tp;
#pragma unroll
    for (int i = 0; i < 10; ++i) { Br[i] = bfrag(pq + (isF1 || i < 6 ? i : 0) * 32 * rs_, rs_); asm volatile("" : "+v"(Br[i])); }
  }

  if (isR) {
    if (w == 0) {
      const int rb = xl >> 1, cb = ri, u = lane & 15;
      unsigned xv = *(const GAS unsigned*)(x + rb * D + cb * 32 + 2 * u);
      int off[8], ho = lane < 16 ? WS_HG + (rb * 320 + cb * 16 + u) * 8 : OOB;
#pragma unroll
      for (int i = 0; i < 8; ++i) off[i] = WS_P2 + (rb * 40 + 2 * cb + (u >> 3)) * 4096 + ((lane >> 4) * 8 + i) * 128 + (u & 7) * 16;
      PIN(xv); PIN(ho);
      v4u o[8];
      gather<8>(rws, off, true, epoch, o);
      f32x2 a[8];
#pragma unroll
      for (int i = 0; i < 8; ++i) a[i] = f32x2{__uint_as_float(o[i][0]), __uint_as_float(o[i][2])};
      const f32x2 s = ((a[0] + a[1]) + (a[2] + a[3])) + ((a[4] + a[5]) + (a[6] + a[7]));
      float s0 = red32(s[0], s[0]), s1 = red32(s[1], s[1]);
      s0 = red16(s0, s0); s1 = red16(s1, s1);
      const v2u o2 = {packbf(bflo(xv) + rbf(s0), bfhi(xv) + rbf(s1)), epoch};
      __builtin_amdgcn_raw_buffer_store_b64(o2, rws, ho, 0, AUX_SC1);
    }
  } else if (isF1) {
    int aa = L_ACT + ar * X2_STR + (w >> 2) * 640 + ak, ago = (lane & 1) ? OOB : WS_AG + (isG ? 0 : 96 * 256) + f0 * 16 + w * 256 + lane * 4, ra = L_RED + ((w & 3) * 64 + lane) * 4;
    PIN(aa); PIN(ago); PIN(ra);
    if (w < 4) {
      const bool a2 = lane < 32;
      int xa = L_ACT + w * X2_STR + lane * 8; PIN(xa);
      const int off[3] = {WS_HG + (w * 160 + lane) * 16, WS_HG + (w * 160 + 64 + lane) * 16, WS_HG + (w * 160 + 128 + (lane & 31)) * 16};
      v4u o[3];
      gather<3>(rws, off, true, epoch, o);
      float v[12]; bool valid[12];
#pragma unroll
      for (int k = 0; k < 3; ++k) {
        const bool a = k < 2 || a2;
        v[4 * k] = a ? bflo(o[k][0]) : 0.f; v[4 * k + 1] = a ? bfhi(o[k][0]) : 0.f; v[4 * k + 2] = a ? bflo(o[k][2]) : 0.f; v[4 * k + 3] = a ? bfhi(o[k][2]) : 0.f;
        valid[4 * k] = valid[4 * k + 1] = valid[4 * k + 2] = valid[4 * k + 3] = a;
      }
      float mb, sb, rs_; ln_stats<12>(v, valid, mb, sb, rs_);
#pragma unroll
      for (int k = 0; k < 3; ++k) if (k < 2 || a2)
        *(LAS v2u*)(L + xa + 512 * k) =
            v2u{packbf(fdiv(rbf(v[4 * k] - mb), sb, rs_), fdiv(rbf(v[4 * k + 1] - mb), sb, rs_)), packbf(fdiv(rbf(v[4 * k + 2] - mb), sb, rs_), fdiv(rbf(v[4 * k + 3] - mb), sb, rs_))};
    }
    __syncthreads();
    f32x4 c = {0.f, 0.f, 0.f, 0.f}, c2 = {0.f, 0.f, 0.f, 0.f};
#pragma unroll
    for (int i = 0; i < 10; i += 2) {
      c = mfma(*(const LAS v4u*)(L + aa + i * 64), Br[i], c);
      c2 = mfma(*(const LAS v4u*)(L + aa + i * 64 + 64), Br[i + 1], c2);
    }
    const float half = spread4(c + c2);
    if (w >= 4) *(LAS float*)(L + ra) = half;
    __syncthreads();
    if (w < 4) {
      const float z = rbf(half + *(const LAS float*)(L + ra));
      const float v = isG ? z * __builtin_amdgcn_rcpf(1.0f + __expf(-z)) : z;
      const float nb = dpp<XOR1>(v);
      const v2u o2 = {packbf(v, nb), epoch};
      __builtin_amdgcn_raw_buffer_store_b64(o2, rws, ago, 0, AUX_SC1);
    }
  } else if (isF2) {
    unsigned hp = 0;
    if (w == 0) {
      const int offh[1] = {WS_HG + (lane >> 4) * 2560 + ((c0 + (lane & 15)) >> 2) * 16};
      v4u oh[1];
      gather<1>(rws, offh, true, epoch, oh);
      hp = (lane & 2) ? oh[0][2] : oh[0][0];
    }
    int aw = L_ACT + ((tid >> 2) & 3) * AS_STR + (tid >> 4) * 32 + (tid & 3) * 8, aa = L_ACT + ar * AS_STR + w * 384 + ak, oo = ((lane >> 4) * D + c0 + (lane & 15)) * 2;
    float hv = (lane & 1) ? bfhi(hp) : bflo(hp);
    PIN(aw); PIN(aa); PIN(oo); PIN(hv);
    const int off[6] = {WS_AG + tid * 16, WS_AG + (512 + tid) * 16, WS_AG + (1024 + tid) * 16, WS_AG + (1536 + tid) * 16, WS_AG + (2048 + tid) * 16, WS_AG + (2560 + tid) * 16};
    v4u o[6];
    gather<6>(rws, off, true, epoch, o);
#pragma unroll
    for (int k = 0; k < 3; ++k)
      *(LAS v2u*)(L + aw + 1024 * k) =
          v2u{packbf(bflo(o[k][0]) * bflo(o[k + 3][0]), bfhi(o[k][0]) * bfhi(o[k + 3][0])), packbf(bflo(o[k][2]) * bflo(o[k + 3][2]), bfhi(o[k][2]) * bfhi(o[k + 3][2]))};
    __syncthreads();
    f32x4 c = {0.f, 0.f, 0.f, 0.f}, c2 = {0.f, 0.f, 0.f, 0.f};
#pragma unroll
    for (int i = 0; i < 6; i += 2) {
      c = mfma(*(const LAS v4u*)(L + aa + i * 64), Br[i], c);
      c2 = mfma(*(const LAS v4u*)(L + aa + i * 64 + 64), Br[i + 1], c2);
    }
    red[w * 64 + lane] = spread4(c + c2);
    __syncthreads();
    if (w == 0) {
      const float s = ((red[lane] + red[64 + lane]) + (red[128 + lane] + red[192 + lane])) + ((red[256 + lane] + red[320 + lane]) + (red[384 + lane] + red[448 + lane]));
      __builtin_amdgcn_raw_buffer_store_b16((u16)bf16bits(hv + rbf(s)), mk(out, 4 * D * 2), oo, 0, 0);
      if (c0 == 0 && lane == 0) __builtin_amdgcn_raw_buffer_store_b32(epoch, rws, WS_E, 0, AUX_SC1);
    }
  }
}

extern "C" void kernel_launch(void* const* d_in, const int* in_sizes, int n_in, void* d_out, int out_size, void* d_ws, size_t ws_size,
                              hipStream_t stream) {
  block_kernel<<<NWG, NTHR, 0, stream>>>((const u16*)d_in[0], (const u16*)d_in[1], (const u16*)d_in[2], (const u16*)d_in[3], (const u16*)d_in[4],
                                         (const u16*)d_in[6], (const u16*)d_in[7], (const u16*)d_in[8], (u16*)d_out, (unsigned char*)d_ws);
}
```
